```python
import torch

def _softmax(x, axis=-1):
    return torch.softmax(x, dim=axis)
def _gelu(x):
    return torch.nn.functional.gelu(x, approximate="tanh")
def _silu(x):
    return torch.nn.functional.silu(x)
def _sigmoid(x):
    return torch.sigmoid(x)
def _layernorm(x, axis=-1, eps=1e-5):
    m = x.mean(dim=axis, keepdim=True)
    v = x.var(dim=axis, keepdim=True, unbiased=False)
    return (x - m) / torch.sqrt(v + eps)
def _scatter_add(tgt, idx, src, axis=0):
    return tgt.clone().index_add_(axis, idx, src)
def _segment_sum(src, seg, n):
    out = torch.zeros((n,) + src.shape[1:], dtype=src.dtype)
    return out.index_add_(0, seg, src)
def _pad(x, pw):
    flat = tuple(v for pair in reversed(pw) for v in pair)
    return torch.nn.functional.pad(x, flat)

kernel_name = 'standalone__paged_block_table_kv_pagedattention__decode'

def setup_inputs():

    _w = (lambda a: (lambda t: t.to(torch.bfloat16) if t.is_floating_point() else t)(torch.as_tensor(a)))
    B, Hq, Hkv, Sq, Skv, D = 1, 32, 8, 1, 65536, 128
    bs = 16
    nbps = max(1, Skv // bs)
    num_phys = B * nbps + 4
    bt = torch.randperm(num_phys)[: B * nbps].reshape(B, nbps).to(torch.int64)
    cl = (nbps * bs - (torch.arange(1, B + 1, dtype=torch.int64) * 2654435761) % max(1, nbps * bs // 2)).to(torch.int64)
    return {
        "q": _w((lambda *s: torch.randn(*s, dtype=torch.bfloat16) * 0.8)(B, Hq, Sq, D)),
        "k_cache": _w((lambda *s: torch.randn(*s, dtype=torch.bfloat16) * 0.8)(num_phys, bs, Hkv, D)),
        "v_cache": _w((lambda *s: torch.randn(*s, dtype=torch.bfloat16) * 0.05)(num_phys, bs, Hkv, D)),
        "block_table": _w(bt),
        "context_lens": _w(cl),
    }

def reference(q, k_cache, v_cache, block_table, context_lens):
    B, Hq, Sq, D = q.shape
    bs = k_cache.shape[1]
    Hkv = k_cache.shape[2]
    nbps = block_table.shape[1]
    Skv = nbps * bs
    bt = block_table.reshape(-1)
    k = (lambda x, *d: x.permute(*d))(k_cache[bt].reshape(B, Skv, Hkv, D), 0, 2, 1, 3)
    v = (lambda x, *d: x.permute(*d))(v_cache[bt].reshape(B, Skv, Hkv, D), 0, 2, 1, 3)
    g = Hq // Hkv
    qg = q.reshape(B, Hkv, g, Sq, D)
    scores = torch.einsum("bhgqd,bhkd->bhgqk", qg, k) * (D ** -0.5)
    pos = torch.arange(Skv)
    msk = pos[None, :] < context_lens[:, None]
    scores = torch.where(msk[:, None, None, None, :], scores, -1e9)
    p = _softmax(scores, axis=-1)
    out = torch.einsum("bhgqk,bhkd->bhgqd", p, v)
    return out.reshape(B, Hq, Sq, D)
```

```cpp
#include <hip/hip_runtime.h>
#include <hip/hip_bf16.h>
#include <cstdio>
#include <cstdint>

using bf16 = __hip_bfloat16;
constexpr int D = 128, PAGE = 16, WAVES = 4, NTHR = WAVES * 64;
constexpr float SCALE = 0.088388347648318440f;
#define ATTN_B 1
#define ATTN_HQ 32
#define ATTN_HKV 8
#define ATTN_CTX 65536
#define ATTN_NSPLIT 64
#define ATTN_COMBINE 0
#define ATTN_KV_NT 1
#define ATTN_PAGED 1
#define ATTN_SQ 1
constexpr int B = ATTN_B, HQ = ATTN_HQ, HKV = ATTN_HKV, CTX = ATTN_CTX, G = HQ / HKV, SQ = ATTN_SQ;
constexpr int GE = G * SQ;
constexpr int GEP = GE <= 4 ? 4 : 8;
constexpr int NBPS = CTX / PAGE;
constexpr int NUM_PHYS = ATTN_PAGED ? B * NBPS + 4 : B * NBPS;
constexpr int nsplit_rule() { int n = 1; while (B * HKV * n < 512 && NBPS / (2 * n) >= 16) n *= 2; return n; }
constexpr int NSPLIT = ATTN_NSPLIT ? ATTN_NSPLIT : nsplit_rule();
constexpr int PPS = NBPS / NSPLIT, PPW = PPS / WAVES;
constexpr int COMBINE = ATTN_COMBINE, KV_NT = ATTN_KV_NT, PAGED = ATTN_PAGED;
constexpr int HPL = GEP / 4;
constexpr int TOK_STRIDE = PAGED ? HKV * D : D;
static_assert(HQ % HKV == 0 && GE >= 1 && GE <= 8, "GE = (HQ/HKV)*SQ in 1..8 (padded to 4 or 8 lane-layout rows); 16 would need acc[128] + x[64] per lane: > 256 VGPRs in this layout");
static_assert(CTX % (PAGE * NSPLIT * WAVES) == 0 && PPW >= 1 && PPW <= 64, "pages split evenly over splits and waves; <= 64 pages per wave (block_table slice in one VGPR)");
constexpr size_t WS_CNT = 0, CNT_BYTES = (size_t)B * HKV * 4, WS_PML = 65536;
constexpr size_t WS_PO = WS_PML + (size_t)B * HQ * SQ * NSPLIT * 2 * 4, WS_END = WS_PO + (size_t)B * HQ * SQ * NSPLIT * D * 4;
constexpr int LDS_P = 0, LDS_MRG = WAVES * 64 * HPL, MRG_REC = 4 + D, LDS_FLAG = LDS_MRG + WAVES * GEP * MRG_REC, LDS_FLOATS = LDS_FLAG + 4;

using bf16x2 = __attribute__((ext_vector_type(2))) __bf16;
using u32x4  = __attribute__((ext_vector_type(4))) unsigned;
using f32x4  = __attribute__((ext_vector_type(4))) float;
using f32x8  = __attribute__((ext_vector_type(8))) float;
__device__ __forceinline__ float dot2(unsigned k, unsigned q, float acc) {
  return __builtin_amdgcn_fdot2_f32_bf16(__builtin_bit_cast(bf16x2, k), __builtin_bit_cast(bf16x2, q), acc, false);
}
__device__ __forceinline__ unsigned cvtpk(float lo, float hi) {
  unsigned r; asm volatile("v_cvt_pk_bf16_f32 %0, %1, %2" : "=v"(r) : "v"(lo), "v"(hi)); return r;
}
template <int CTRL> __device__ __forceinline__ float dpp(float x) {
  return __builtin_bit_cast(float, __builtin_amdgcn_mov_dpp(__builtin_bit_cast(int, x), CTRL, 0xf, 0xf, true));
}
constexpr int XOR1 = 0xB1, XOR2 = 0x4E, XOR7 = 0x141, XOR8 = 0x128, ROR4 = 0x124;
__device__ __forceinline__ float xrow16_max(float x) {
  auto s = __builtin_amdgcn_permlane16_swap(__float_as_uint(x), __float_as_uint(x), false, false);
  x = fmaxf(__uint_as_float(s[0]), __uint_as_float(s[1]));
  auto t = __builtin_amdgcn_permlane32_swap(__float_as_uint(x), __float_as_uint(x), false, false);
  return fmaxf(__uint_as_float(t[0]), __uint_as_float(t[1]));
}
__device__ __forceinline__ float xrow16_sum(float x) {
  auto s = __builtin_amdgcn_permlane16_swap(__float_as_uint(x), __float_as_uint(x), false, false);
  x = __uint_as_float(s[0]) + __uint_as_float(s[1]);
  auto t = __builtin_amdgcn_permlane32_swap(__float_as_uint(x), __float_as_uint(x), false, false);
  return __uint_as_float(t[0]) + __uint_as_float(t[1]);
}
template <typename TIn> struct Stage;
template <> struct Stage<bf16> { using T = u32x4;
  __device__ static __forceinline__ T ld8(const bf16* p) { return KV_NT ? __builtin_nontemporal_load(reinterpret_cast<const u32x4*>(p)) : *reinterpret_cast<const u32x4*>(p); }
  __device__ static __forceinline__ T ld8q(const bf16* p) { return *reinterpret_cast<const u32x4*>(p); }
  __device__ static __forceinline__ u32x4 tobf(T x) { return x; } };
template <> struct Stage<float> { using T = f32x8;
  __device__ static __forceinline__ T ld8(const float* p) { return KV_NT ? __builtin_nontemporal_load(reinterpret_cast<const f32x8*>(p)) : *reinterpret_cast<const f32x8*>(p); }
  __device__ static __forceinline__ T ld8q(const float* p) { return *reinterpret_cast<const f32x8*>(p); }
  __device__ static __forceinline__ u32x4 tobf(T x) { u32x4 w = {cvtpk(x[0], x[1]), cvtpk(x[2], x[3]), cvtpk(x[4], x[5]), cvtpk(x[6], x[7])}; return w; } };

template <typename TIn> struct DecArgs { const TIn* q; const TIn* k; const TIn* v; const int* bt; const int* lens; bf16* out; unsigned char* ws; };

__device__ __forceinline__ void st_partial(float2* p, float2 v) {
  if constexpr (COMBINE == 1) __hip_atomic_store(reinterpret_cast<unsigned long long*>(p), __builtin_bit_cast(unsigned long long, v), __ATOMIC_RELAXED, __HIP_MEMORY_SCOPE_AGENT);
  else *p = v;
}
__device__ __forceinline__ void merge_partials(const float* pml, const float* po, int n, int d0, bf16* o) {
  float M = -INFINITY;
  for (int s = 0; s < n; ++s) M = fmaxf(M, pml[2 * s]);
  float L = 0.f, o0 = 0.f, o1 = 0.f;
  for (int s = 0; s < n; ++s) {
    const float ls = pml[2 * s + 1], w = (ls > 0.f) ? __builtin_amdgcn_exp2f(pml[2 * s] - M) : 0.f;
    const float2 a = *reinterpret_cast<const float2*>(po + (size_t)s * D + d0);
    L = fmaf(w, ls, L); o0 = fmaf(w, a.x, o0); o1 = fmaf(w, a.y, o1);
  }
  const float inv = (L > 0.f) ? 1.f / L : 0.f;
  *reinterpret_cast<unsigned*>(o + d0) = cvtpk(o0 * inv, o1 * inv);
}

template <typename TIn>
__global__ void __launch_bounds__(NTHR) attn_decode_split(DecArgs<TIn> a) {
  __shared__ __attribute__((aligned(16))) float lds[LDS_FLOATS];
  const int s = blockIdx.x, kvh = blockIdx.y, b = blockIdx.z;
  const int tid = threadIdx.x, lane = tid & 63, wave = __builtin_amdgcn_readfirstlane(tid >> 6), r = lane >> 4, c = lane & 15;
  constexpr float C2 = SCALE * 1.4426950408889634f;
  const int len = PAGED ? a.lens[b] : CTX;
  const int npages = (len + PAGE - 1) / PAGE;
  const int pps = (npages + NSPLIT - 1) / NSPLIT;
  const int p0 = s * pps, p1 = min(p0 + pps, npages);
  const int nwp = (p1 > p0 + wave) ? min(PPW, (p1 - p0 - wave + WAVES - 1) / WAVES) : 0;
  int btv = 0;
  if (lane < PPW) { const int pg = min(p0 + wave + 4 * lane, NBPS - 1); btv = PAGED ? a.bt[(size_t)b * NBPS + pg] : pg; }
  u32x4 qpk[GEP];
#pragma unroll
  for (int g = 0; g < GEP; ++g) qpk[g] = g < GE ? Stage<TIn>::tobf(Stage<TIn>::ld8q(a.q + ((size_t)b * HQ * SQ + kvh * GE + g) * D + 8 * c)) : u32x4{0u, 0u, 0u, 0u};
  const size_t loff = (size_t)r * TOK_STRIDE + 8 * c;
#define PAGE_BASE(phys) (PAGED ? (size_t)(phys) * (PAGE * HKV * D) + (size_t)kvh * D : ((size_t)(b * HKV + kvh) * CTX + (size_t)(phys) * PAGE) * D)
#define ISSUE(i_) do { const int phys_ = __builtin_amdgcn_readlane(btv, (i_)); const TIn* kb_ = a.k + PAGE_BASE(phys_) + loff; const TIn* vb_ = a.v + PAGE_BASE(phys_) + loff; \
    _Pragma("unroll") for (int t_ = 0; t_ < 4; ++t_) { Kn[t_] = Stage<TIn>::ld8(kb_ + (size_t)t_ * 4 * TOK_STRIDE); Vn[t_] = Stage<TIn>::ld8(vb_ + (size_t)t_ * 4 * TOK_STRIDE); } } while (0)
  typename Stage<TIn>::T Kn[4], Vn[4];
  float m[HPL], l[HPL], acc[GEP * 8];
#pragma unroll
  for (int j = 0; j < HPL; ++j) { m[j] = -INFINITY; l[j] = 0.f; }
#pragma unroll
  for (int k = 0; k < GEP * 8; ++k) acc[k] = 0.f;
  if (nwp > 0) ISSUE(0);
  for (int i = 0; i < nwp; ++i) {
    u32x4 Kc[4], Vc[4];
#pragma unroll
    for (int t = 0; t < 4; ++t) { Kc[t] = Stage<TIn>::tobf(Kn[t]); Vc[t] = Stage<TIn>::tobf(Vn[t]); }
    const int pg = p0 + wave + 4 * i, rem = len - pg * PAGE;
    if (i + 1 < nwp) ISSUE(i + 1);
    float x[4 * GEP];
#pragma unroll
    for (int t = 0; t < 4; ++t)
#pragma unroll
      for (int g = 0; g < GEP; ++g) { float d = 0.f;
        if (g < GE) {
#pragma unroll
          for (int k = 0; k < 4; ++k) d = dot2(Kc[t][k], qpk[g][k], d); }
        x[t * GEP + g] = d; }
#define TR_STEP(HALF, CTRL, BIT) _Pragma("unroll") for (int h = 0; h < (HALF); ++h) _Pragma("unroll") for (int j = 0; j < HPL; ++j) { \
      const float lo_ = x[h * HPL + j], hi_ = x[(h + (HALF)) * HPL + j]; const float t1_ = lo_ + dpp<CTRL>(lo_), t2_ = hi_ + dpp<CTRL>(hi_); \
      x[h * HPL + j] = (c & (BIT)) ? t2_ : t1_; }
    TR_STEP(8, XOR8, 8) TR_STEP(4, XOR7, 4) TR_STEP(2, XOR2, 2) TR_STEP(1, XOR1, 1)
#undef TR_STEP
    const bool tail = rem < PAGE;
    const bool live = !tail || (r + 4 * (c >> 2)) < rem;
    float p[HPL], alpha[HPL];
#pragma unroll
    for (int j = 0; j < HPL; ++j) {
      const float sc = live ? x[j] * C2 : -INFINITY;
      float pm = sc; pm = fmaxf(pm, dpp<ROR4>(pm)); pm = fmaxf(pm, dpp<XOR8>(pm)); pm = xrow16_max(pm);
      const float mn = fmaxf(m[j], pm), msub = (mn == -INFINITY) ? 0.f : mn;
      alpha[j] = __builtin_amdgcn_exp2f(m[j] - msub); p[j] = __builtin_amdgcn_exp2f(sc - msub);
      l[j] = fmaf(l[j], alpha[j], p[j]); m[j] = mn;
    }
#pragma unroll
    for (int q4 = 0; q4 < 4; ++q4)
#pragma unroll
      for (int j = 0; j < HPL; ++j) { if (q4 * HPL + j >= GE) continue;
        const float al = q4 == 0 ? dpp<0x00>(alpha[j]) : q4 == 1 ? dpp<0x55>(alpha[j]) : q4 == 2 ? dpp<0xAA>(alpha[j]) : dpp<0xFF>(alpha[j]);
#pragma unroll
        for (int k = 0; k < 8; ++k) acc[(q4 * HPL + j) * 8 + k] *= al; }
    float* prow = lds + LDS_P + (wave * 4 + r) * 16 * HPL;
#pragma unroll
    for (int j = 0; j < HPL; ++j) prow[c * HPL + j] = p[j];
    asm volatile("s_waitcnt lgkmcnt(0)" ::: "memory");
    f32x4 pr[GEP];
#pragma unroll
    for (int u = 0; u < GEP; ++u) pr[u] = *reinterpret_cast<const f32x4*>(prow + 4 * u);
    if (tail) {
#pragma unroll
      for (int t = 0; t < 4; ++t) { const bool lv = (r + 4 * t) < rem;
#pragma unroll
        for (int k = 0; k < 4; ++k) Vc[t][k] = lv ? Vc[t][k] : 0u; } }
#pragma unroll
    for (int t = 0; t < 4; ++t) {
      float vf[8];
#pragma unroll
      for (int k = 0; k < 4; ++k) { vf[2 * k] = __builtin_bit_cast(float, Vc[t][k] << 16); vf[2 * k + 1] = __builtin_bit_cast(float, Vc[t][k] & 0xffff0000u); }
#pragma unroll
      for (int g = 0; g < GE; ++g) { const float pg_ = pr[(t * GEP + g) >> 2][(t * GEP + g) & 3];
#pragma unroll
        for (int k = 0; k < 8; ++k) acc[g * 8 + k] = fmaf(pg_, vf[k], acc[g * 8 + k]); } }
  }
#undef ISSUE
#pragma unroll
  for (int k = 0; k < GE * 8; ++k) acc[k] = xrow16_sum(acc[k]);
#pragma unroll
  for (int j = 0; j < HPL; ++j) { float t = l[j]; t += dpp<ROR4>(t); t += dpp<XOR8>(t); l[j] = xrow16_sum(t); }
  float* mrg = lds + LDS_MRG;
  if (r == 0) {
#pragma unroll
    for (int g = 0; g < GE; ++g) {
      float* rec = mrg + (wave * GEP + g) * MRG_REC;
#pragma unroll
      for (int k = 0; k < 8; k += 4) *reinterpret_cast<f32x4*>(rec + 4 + 8 * c + k) = f32x4{acc[g * 8 + k], acc[g * 8 + k + 1], acc[g * 8 + k + 2], acc[g * 8 + k + 3]};
      if (c == g / HPL) { rec[0] = m[g % HPL]; rec[1] = l[g % HPL]; } } }
  __syncthreads();
  float* pml = reinterpret_cast<float*>(a.ws + WS_PML);
  float* po = reinterpret_cast<float*>(a.ws + WS_PO);
#pragma unroll
  for (int jj = 0; jj < HPL; ++jj) {
    const int g = wave + 4 * jj, d0 = 2 * lane;
    if (g >= GE) continue;
    const size_t row = (size_t)b * HQ * SQ + kvh * GE + g;
    float M = -INFINITY;
#pragma unroll
    for (int w = 0; w < WAVES; ++w) M = fmaxf(M, mrg[(w * GEP + g) * MRG_REC]);
    float L = 0.f, o0 = 0.f, o1 = 0.f;
#pragma unroll
    for (int w = 0; w < WAVES; ++w) { const float* rec = mrg + (w * GEP + g) * MRG_REC; const float lw = rec[1];
      const float wt = (lw > 0.f) ? __builtin_amdgcn_exp2f(rec[0] - M) : 0.f;
      L = fmaf(wt, lw, L); o0 = fmaf(wt, rec[4 + d0], o0); o1 = fmaf(wt, rec[4 + d0 + 1], o1); }
    if constexpr (NSPLIT == 1) {
      const float inv = (L > 0.f) ? 1.f / L : 0.f;
      *reinterpret_cast<unsigned*>(a.out + row * D + d0) = cvtpk(o0 * inv, o1 * inv);
    } else {
      const size_t slot = row * NSPLIT + s;
      st_partial(reinterpret_cast<float2*>(po + slot * D + d0), make_float2(o0, o1));
      if (lane == 0) st_partial(reinterpret_cast<float2*>(pml + 2 * slot), make_float2(M, L));
    }
  }
  if constexpr (COMBINE == 1 && NSPLIT > 1) {
    asm volatile("s_waitcnt vmcnt(0)" ::: "memory");
    __syncthreads();
    if (tid == 0) {
      unsigned* cnt = reinterpret_cast<unsigned*>(a.ws + WS_CNT) + b * HKV + kvh;
      const unsigned old = __hip_atomic_fetch_add(cnt, 1u, __ATOMIC_RELAXED, __HIP_MEMORY_SCOPE_AGENT);
      const unsigned last = (old == (unsigned)(NSPLIT - 1)) ? 1u : 0u;
      if (last) __builtin_amdgcn_fence(__ATOMIC_ACQUIRE, "agent");
      reinterpret_cast<unsigned*>(lds + LDS_FLAG)[0] = last;
    }
    __syncthreads();
    if (reinterpret_cast<const unsigned*>(lds + LDS_FLAG)[0] == 0u) return;
#pragma unroll
    for (int jj = 0; jj < HPL; ++jj) { if (wave + 4 * jj >= GE) continue;
      const size_t row = (size_t)b * HQ * SQ + kvh * GE + wave + 4 * jj;
      merge_partials(pml + row * NSPLIT * 2, po + row * NSPLIT * D, NSPLIT, 2 * lane, a.out + row * D);
    }
  }
}

__global__ void __launch_bounds__(64) attn_decode_combine(const float* pml, const float* po, bf16* out) {
  const size_t row = (size_t)blockIdx.y * HQ * SQ + blockIdx.x;
  merge_partials(pml + row * NSPLIT * 2, po + row * NSPLIT * D, NSPLIT, 2 * (threadIdx.x & 63), out + row * D);
}

template <typename TIn>
static void attn_decode_launch(const TIn* q, const TIn* k, const TIn* v, const int* bt, const int* lens, bf16* out, void* ws, hipStream_t stream) {
  DecArgs<TIn> a{q, k, v, bt, lens, out, (unsigned char*)ws};
  if (COMBINE == 1 && NSPLIT > 1 && hipMemsetAsync((char*)ws + WS_CNT, 0, CNT_BYTES, stream) != hipSuccess) { fprintf(stderr, "kernel_launch: memset failed\n"); return; }
  hipLaunchKernelGGL(attn_decode_split<TIn>, dim3(NSPLIT, HKV, B), dim3(NTHR), 0, stream, a);
  if (COMBINE == 0 && NSPLIT > 1) hipLaunchKernelGGL(attn_decode_combine, dim3(HQ * SQ, B), dim3(64), 0, stream, (const float*)((char*)ws + WS_PML), (const float*)((char*)ws + WS_PO), out);
  const hipError_t le = hipPeekAtLastError();
  if (le != hipSuccess) fprintf(stderr, "kernel_launch: launch failed: %s\n", hipGetErrorName(le));
}

extern "C" void kernel_launch(void* const* d_in, const int* in_sizes, int n_in, void* d_out, int out_size, void* d_ws, size_t ws_size, hipStream_t stream) {
  attn_decode_launch<bf16>((const bf16*)d_in[0], (const bf16*)d_in[1], (const bf16*)d_in[2], (const int*)d_in[3], (const int*)d_in[4], (bf16*)d_out, d_ws, stream);
}
```

```cpp
#include <hip/hip_runtime.h>
#include <hip/hip_bf16.h>
#include <stdint.h>
#include <type_traits>

#define GQ 4
#define TBPS 7.52
#define LEAD 70
#define G0 5
#define EXS 320
#define PF 16
#define PGAP 10
#define BCH 4

using u32x2 = __attribute__((ext_vector_type(2))) unsigned;
using u32x4 = __attribute__((ext_vector_type(4))) unsigned;
using f32x4 = __attribute__((ext_vector_type(4))) float;
using bf16x8 = __attribute__((ext_vector_type(8))) __bf16;
using s16x4 = __attribute__((ext_vector_type(4))) short;
typedef unsigned long long u64;
#define GAS __attribute__((address_space(1)))
#define LAS __attribute__((address_space(3)))

constexpr int HKV = 8, G = 4, D = 128, NBPS = 4096, SKV = 65536, NS = 32;
constexpr int NW = 3, NWT = NW + 1;
constexpr int UST = NS * NW;
constexpr int NT = (SKV / 4 + UST * 64 - 1) / (UST * 64);
constexpr unsigned PAGE_BYTES = 32768;
constexpr int GMAX = GQ, BLK = GQ * UST / 4, P0 = G0 * BLK;
constexpr int IDL = 44, IDS = IDL * 96;
constexpr int NCH = (NBPS - P0 + 255) / 256;
constexpr unsigned TAUC = (unsigned)(2048.0 * 256 * NW / (TBPS * 1e12) * 1e8 * 256);
constexpr unsigned WS_EPOCH = 0, WS_VAL = 4096, WS_END = WS_VAL + HKV * 32 * NS * 128;

template <int CTRL> __device__ __forceinline__ float dpp(float x) {
  return __builtin_bit_cast(float, __builtin_amdgcn_mov_dpp(__builtin_bit_cast(int, x), CTRL, 0xf, 0xf, true));
}
template <int BANK> __device__ __forceinline__ float bank_sel(float old, float x) {
  return __builtin_bit_cast(float, __builtin_amdgcn_update_dpp(__builtin_bit_cast(int, old), __builtin_bit_cast(int, x), 0xE4, 0xf, 1 << BANK, false));
}
__device__ __forceinline__ float xrow_max(float x) {
  auto s = __builtin_amdgcn_permlane16_swap(__float_as_uint(x), __float_as_uint(x), false, false);
  x = fmaxf(__uint_as_float(s[0]), __uint_as_float(s[1]));
  auto t = __builtin_amdgcn_permlane32_swap(__float_as_uint(x), __float_as_uint(x), false, false);
  return fmaxf(__uint_as_float(t[0]), __uint_as_float(t[1]));
}
__device__ __forceinline__ float xrow_sum(float x) {
  auto s = __builtin_amdgcn_permlane16_swap(__float_as_uint(x), __float_as_uint(x), false, false);
  x = __uint_as_float(s[0]) + __uint_as_float(s[1]);
  auto t = __builtin_amdgcn_permlane32_swap(__float_as_uint(x), __float_as_uint(x), false, false);
  return __uint_as_float(t[0]) + __uint_as_float(t[1]);
}
__device__ __forceinline__ unsigned cvtpk(float lo, float hi) {
  using bf16x2 = __attribute__((ext_vector_type(2))) __bf16;
  using f32x2 = __attribute__((ext_vector_type(2))) float;
  return __builtin_bit_cast(unsigned, __builtin_convertvector(f32x2{lo, hi}, bf16x2));
}

template <int CTRL, int ROWS> __device__ __forceinline__ unsigned dpp_add(unsigned x) { return x + __builtin_amdgcn_update_dpp(0u, x, CTRL, ROWS, 0xf, false); }

__device__ __forceinline__ void combine_job(const char* ws, int h, int job, unsigned tag, __hip_bfloat16* out, int lane) {
  const u32x4 rs = {(unsigned)(u64)ws, (unsigned)((u64)ws >> 32), WS_END, 0x00020000u};
  unsigned vo[4];
  float v0[4], v1[4], ls[4];
#pragma unroll
  for (int k = 0; k < 4; ++k) { vo[k] = WS_VAL + (h * 32 + job) * NS * 128 + k * 1024 + lane * 16; }
  u32x4 x0, x1, x2, x3, y0, y1, y2, y3; unsigned t; int spins = 1 << 17;
#define RD(r, k) "buffer_load_dwordx4 v[" #r ":" #r "+3], %[o" #k "], %[rs], 0 offen sc1\n\t"
#define CK(r, k) "v_perm_b32 %[t], v[" #r "+1], v[" #r "+3], %[hi]\n\tv_cmp_eq_u32 vcc, %[tag], %[t]\n\tv_perm_b32 %[t], v[" #r "+1], v[" #r "+3], %[lo]\n\ts_nop 1\n\t" \
    "v_cndmask_b32 %[a" #k "], %[a" #k "], v[" #r "], vcc\n\tv_cndmask_b32 %[b" #k "], %[b" #k "], v[" #r "+2], vcc\n\t" \
    "v_cndmask_b32 %[l" #k "], %[l" #k "], %[t], vcc\n\tv_cndmask_b32_e64 %[o" #k "], %[o" #k "], -16, vcc\n\t"
#define ROUND(r0, r1, r2, r3) "s_waitcnt vmcnt(4)\n\t" CK(r0, 0) CK(r1, 1) CK(r2, 2) CK(r3, 3) \
    "v_and_b32 %[t], %[o0], %[o1]\n\tv_and_b32 %[t], %[t], %[o2]\n\tv_and_b32 %[t], %[t], %[o3]\n\tv_cmp_lt_i32 vcc, -1, %[t]\n\ts_cbranch_vccz 3f\n\t" RD(r0, 0) RD(r1, 1) RD(r2, 2) RD(r3, 3)
  asm volatile(RD(64, 0) RD(68, 1) RD(72, 2) RD(76, 3) "s_sleep %[gap]\n\t" RD(80, 0) RD(84, 1) RD(88, 2) RD(92, 3)
               "2:\n\t" ROUND(64, 68, 72, 76) ROUND(80, 84, 88, 92) "s_sub_u32 %[n], %[n], 1\n\ts_cbranch_scc0 2b\n3:"
               : "=&{v[64:67]}"(x0), "=&{v[68:71]}"(x1), "=&{v[72:75]}"(x2), "=&{v[76:79]}"(x3), "=&{v[80:83]}"(y0), "=&{v[84:87]}"(y1), "=&{v[88:91]}"(y2), "=&{v[92:95]}"(y3),
                 [t] "=&v"(t), [n] "+s"(spins), [o0] "+v"(vo[0]), [o1] "+v"(vo[1]), [o2] "+v"(vo[2]), [o3] "+v"(vo[3]),
                 [a0] "=&v"(v0[0]), [a1] "=&v"(v0[1]), [a2] "=&v"(v0[2]), [a3] "=&v"(v0[3]), [b0] "=&v"(v1[0]), [b1] "=&v"(v1[1]), [b2] "=&v"(v1[2]), [b3] "=&v"(v1[3]),
                 [l0] "=&v"(ls[0]), [l1] "=&v"(ls[1]), [l2] "=&v"(ls[2]), [l3] "=&v"(ls[3])
               : [rs] "s"(rs), [tag] "s"(tag), [hi] "s"(0x07060302), [lo] "s"(0x05040100), [gap] "n"(PGAP) : "vcc", "scc", "memory");
  float M = fmaxf(fmaxf(ls[0], ls[1]), fmaxf(ls[2], ls[3]));
  M = fmaxf(M, dpp<0x128>(M)); M = xrow_max(M);
  float W = 0.f, o0 = 0.f, o1 = 0.f;
#pragma unroll
  for (int k = 0; k < 4; ++k) { const float w = __builtin_amdgcn_exp2f(ls[k] - M); W += w; o0 = fmaf(w, v0[k], o0); o1 = fmaf(w, v1[k], o1); }
  W += dpp<0x128>(W); o0 += dpp<0x128>(o0); o1 += dpp<0x128>(o1);
  W = xrow_sum(W); o0 = xrow_sum(o0); o1 = xrow_sum(o1);
  const float r = __builtin_amdgcn_rcpf(W);
  if (lane < 8) *reinterpret_cast<unsigned*>(out + (h * G + (job >> 3)) * D + (job & 7) * 16 + lane * 2) = cvtpk(o0 * r, o1 * r);
  asm volatile("s_waitcnt vmcnt(1)" :: "{v[64:67]}"(x0), "{v[68:71]}"(x1), "{v[72:75]}"(x2), "{v[76:79]}"(x3),
               "{v[80:83]}"(y0), "{v[84:87]}"(y1), "{v[88:91]}"(y2), "{v[92:95]}"(y3));
}

__global__ void __launch_bounds__(NWT * 64, 2) attn_main(const char* __restrict__ Q, const char* __restrict__ K, const char* __restrict__ V, const int* __restrict__ bt,
                                                        const int* __restrict__ cl, __hip_bfloat16* __restrict__ out, char* ws, unsigned kvbytes) {
  __shared__ __attribute__((aligned(16))) float lds[NW * G * 132 + NW * G * 2 + 4];
  __shared__ u32x4 flg[IDS / 16];
  __shared__ u32x2 rnk[256];
  __shared__ unsigned short lst[NBPS];
  unsigned pf0, pf1;
  { u64 pc; unsigned span;
    asm volatile("s_getpc_b64 %0\n8:\n\ts_mov_b32 %1, 9f-8b-4" : "=s"(pc), "=s"(span));
    const unsigned o = (threadIdx.x & 63) * 128;
    asm volatile("s_cmp_lt_u32 %5, %6\n\ts_cbranch_scc0 1f\n\tglobal_load_dword %0, %2, %4\n\tglobal_load_dword %1, %3, %4\n1:"
                 : "=&v"(pf0), "=&v"(pf1) : "v"(min(o, span)), "v"(min(o + 8192, span)), "s"(pc),
                   "s"(blockIdx.x | (__builtin_amdgcn_readfirstlane(threadIdx.x >> 6) == NW ? 0u : 1024u)), "n"(PF) : "scc"); }
  asm volatile("" :: "s"(Q), "s"(K), "s"(V), "s"(bt), "s"(cl), "s"(out), "s"(ws), "s"(kvbytes));
  const int lane = threadIdx.x & 63, wave = __builtin_amdgcn_readfirstlane(threadIdx.x >> 6), bid = blockIdx.x;
  const int h = bid >> 5, s = bid & 31;
  const auto wr = __builtin_amdgcn_make_buffer_rsrc((void*)ws, 0, WS_END, 0x00020000);
  GAS unsigned* ep = (GAS unsigned*)(ws + WS_EPOCH + h * 256);
  const unsigned gv = WS_VAL + ((h * 32 + (lane >> 3)) * NS + s) * 128 + (lane & 7) * 16;
  if (wave == NW) {
    u32x4 tv[NCH];
#pragma unroll
    for (int k = 0; k < NCH; ++k) asm volatile("global_load_dwordx4 %0, %1, %2" : "=v"(tv[k]) : "v"(min(P0 + 256 * k + 4 * lane, NBPS - 4) * 4), "s"(bt));
    unsigned e;
    asm volatile("global_load_dword %0, %1, %2 sc1" : "=v"(e) : "v"(0), "s"(ep));
    {
      const u32x4 rw = {(unsigned)(u64)ws, (unsigned)((u64)ws >> 32), WS_END, 0x00020000u};
#pragma unroll
      for (int g = 0; g < G; ++g) asm volatile("buffer_store_dwordx4 %0, %1, %2, %3 offen sc1\n\ts_nop 1" :: "v"(u32x4{0, 0, 0, 0}), "v"(gv), "s"(rw), "s"(g * 8 * NS * 128) : "memory");
    }
    if (lane == 0) lds[NW * G * 134 + 1] = __int_as_float(-1);
    int len = *(const __attribute__((address_space(4))) int*)cl;
#pragma unroll
    for (int k = 0; k < IDS / 16; k += 64) flg[min(k + lane, IDS / 16 - 1)] = u32x4{0, 0, 0, 0};
    len = len <= 0 ? SKV : min(len, SKV);
    const int n4 = kvbytes <= IDS * PAGE_BYTES ? max(((len + 15) >> 4) - 1 - P0, 0) / BLK * (BLK / 4) : 0;
    asm volatile("s_waitcnt lgkmcnt(0)\n\ts_barrier" ::: "memory");
#pragma unroll
    for (int k = 0; k < NCH; ++k) {
      asm volatile("s_waitcnt vmcnt(%1)" : "+v"(tv[k]) : "n"(NCH - 1 - k + 1 + G));
      if (k == 0) asm volatile("" :: "v"(pf0), "v"(pf1));
      if (__builtin_expect(lane < n4 - 64 * k, 1)) {
#pragma unroll
        for (int c = 0; c < 4; ++c) reinterpret_cast<unsigned char*>(flg)[tv[k][c]] = 1;
      }
    }
    asm volatile("" ::: "memory");
    const int bl = min(lane, IDL - 1);
    unsigned w[3], n[3];
#pragma unroll
    for (int j = 0; j < 3; ++j) {
      const u32x4 f0 = flg[6 * bl + 2 * j], f1 = flg[6 * bl + 2 * j + 1];
      const unsigned b0 = __builtin_amdgcn_udot4(f0[1], 0x80402010u, __builtin_amdgcn_udot4(f0[0], 0x08040201u, 0u, false), false);
      const unsigned b1 = __builtin_amdgcn_udot4(f0[3], 0x80402010u, __builtin_amdgcn_udot4(f0[2], 0x08040201u, 0u, false), false);
      const unsigned b2 = __builtin_amdgcn_udot4(f1[1], 0x80402010u, __builtin_amdgcn_udot4(f1[0], 0x08040201u, 0u, false), false);
      const unsigned b3 = __builtin_amdgcn_udot4(f1[3], 0x80402010u, __builtin_amdgcn_udot4(f1[2], 0x08040201u, 0u, false), false);
      w[j] = b0 | b1 << 8 | b2 << 16 | b3 << 24;
      n[j] = (j ? n[j - 1] : 0u) + __popc(w[j]);
    }
    unsigned sc = n[2];
    sc = dpp_add<0x111, 0xf>(sc); sc = dpp_add<0x112, 0xf>(sc); sc = dpp_add<0x114, 0xf>(sc); sc = dpp_add<0x118, 0xf>(sc);
    sc = dpp_add<0x142, 0xa>(sc); sc = dpp_add<0x143, 0xc>(sc);
    const int total = __builtin_amdgcn_readlane(sc, IDL - 1);
    const unsigned ex = 2 * (P0 + sc - n[2]);
    if (lane < IDL) { rnk[3 * lane] = u32x2{w[0], ex}; rnk[3 * lane + 1] = u32x2{w[1], ex + 2 * n[0]}; rnk[3 * lane + 2] = u32x2{w[2], ex + 2 * n[1]}; }
    asm volatile("" ::: "memory");
#pragma unroll
    for (int b = 0; b < NCH; b += BCH) if (__builtin_expect(64 * b < n4, 1)) {
      u32x2 r[4 * BCH];
#pragma unroll
      for (int i = 0; i < 4 * BCH; ++i) if (b + i / 4 < NCH) r[i] = rnk[(tv[b + i / 4][i & 3] >> 5) & 255];
#pragma unroll
      for (int i = 0; i < 4 * BCH; ++i) if (b + i / 4 < NCH) asm volatile("" : "+v"(r[i]));
#pragma unroll
      for (int k = b; k < b + BCH && k < NCH; ++k) if (__builtin_expect(lane < n4 - 64 * k, 1)) {
#pragma unroll
        for (int c = 0; c < 4; ++c) { const u32x2 x = r[4 * (k - b) + c]; *reinterpret_cast<unsigned short*>(reinterpret_cast<char*>(lst) + (x[1] + 2 * __popc(__builtin_amdgcn_ubfe(x[0], 0, tv[k][c])))) = (unsigned short)tv[k][c]; }
      }
    }
    asm volatile("s_waitcnt lgkmcnt(0)" ::: "memory");
    if (lane == 0) lds[NW * G * 134 + 1] = __int_as_float(total == 4 * n4 ? P0 + total : 0);
    asm volatile("s_waitcnt vmcnt(0)" : "+v"(e) :: "memory");
    if (lane == 0) lds[NW * G * 134] = __uint_as_float((((e + 1u) & 0x7fffu) | 0x8000u) * 0x10001u);
  } else {
    auto stream = [&](auto gn_) __attribute__((always_inline)) {
      constexpr int GN = decltype(gn_)::value;
      u64 tstart; asm volatile("s_memrealtime %0" : "=s"(tstart));
      __builtin_amdgcn_sched_barrier(0);
      const int u0 = s + NS * wave;
      int len = *(const __attribute__((address_space(4))) int*)cl;
      unsigned tb[NT]; u32x4 qr;
#pragma unroll
      for (int i = 0; i < NT; ++i) asm volatile("global_load_dword %0, %1, %2" : "=v"(tb[i]) : "v"(min((u0 + UST * (lane + 64 * i)) >> 2, NBPS - 1) * 4), "s"(bt));
      asm volatile("global_load_dwordx4 %0, %1, %2" : "=v"(qr) : "v"((h * G + (lane & 3)) * 256 + (lane >> 4) * 64 + ((lane >> 2) & 3) * 16), "s"(Q));
      __builtin_amdgcn_sched_barrier(0);
      float c2 = 0.088388347648318440f * 1.4426950408889634f;
      if (len <= 0) { len = SKV; c2 = 0.f; }
      len = min(len, SKV);
      const int nq = (len + 3) >> 2;
      const int nu = u0 < nq ? (nq - u0 + UST - 1) / UST : 0;
      const int nit = max((nu + GN - 1) / GN, 1), shortn = nu - GN * (nit - 1), ip = nit > G0 + 1 ? G0 + u0 * (nit - 1 - G0) / UST : nit - 1;
      int cnt = min(nu, GN);
      const unsigned sub = (u0 & 3) * 8192 + h * 256;
      const unsigned voffK = sub + ((lane >> 2) & 3) * 2048 + (lane >> 4) * 64 + (lane & 3) * 16;
      const unsigned voffV = sub + (lane >> 4) * 2048 + (lane & 15) * 16;
      asm volatile("s_waitcnt vmcnt(%1)" : "+v"(tb[0]) : "n"(NT));
      tb[0] *= PAGE_BYTES;
      const u64 ka = (u64)K, va = (u64)V;
      u32x4 kb[GN], vb[GN];
      unsigned so[GN], nr[GN];
#define LOADI(dst, voff, base, t) do { const u32x4 r_ = {(unsigned)(base), (unsigned)((base) >> 32), nr[t], 0x00020000u}; \
        asm volatile("buffer_load_dwordx4 %0, %1, %2, %3 offen nt" : "=v"(dst) : "v"(voff), "s"(r_), "s"(so[t])); } while (0)
#define LOAD(dst, voff, base, t) do { const u32x4 r_ = {(unsigned)(base), (unsigned)((base) >> 32), nr[t], 0x00020000u}; \
        asm volatile("buffer_load_dwordx4 %0, %1, %2, %3 offen nt" : "+v"(dst) : "v"(voff), "s"(r_), "s"(so[t])); } while (0)
#define WAIT(x) asm volatile("s_waitcnt vmcnt(%1)" : "+v"(x) : "n"(2 * GN - 1))
#define SETTLE() do { _Pragma("unroll") for (int t = 0; t < GN; ++t) asm volatile("" : "+s"(so[t])); asm volatile("s_nop 4"); } while (0)
#pragma unroll
      for (int t = 0; t < GN; ++t) { so[t] = __builtin_amdgcn_readlane(tb[0], t); nr[t] = t < cnt ? kvbytes : 0u; }
      SETTLE();
#pragma unroll
      for (int t = 0; t < GN; ++t) LOADI(kb[t], voffK, ka, t);
#pragma unroll
      for (int t = 0; t < GN; ++t) LOADI(vb[t], voffV, va, t);
      asm volatile("s_barrier" ::: "memory");
      __builtin_amdgcn_sched_barrier(0);
      asm volatile("s_waitcnt vmcnt(%1)" : "+v"(qr) : "n"(2 * GN));
#pragma unroll
      for (int i = 1; i < NT; ++i) { asm volatile("" : "+v"(tb[i])); tb[i] *= PAGE_BYTES; }
      const bf16x8 qf = __builtin_bit_cast(bf16x8, qr);
      const unsigned c = (lane >> 2) & 3;
      const unsigned mask0 = c == 0 ? 0x0000ffffu : c == 1 ? 0xffff0000u : 0u, mask1 = c == 2 ? 0x0000ffffu : c == 3 ? 0xffff0000u : 0u;
      float m = -3.0e38f, l = 0.f;
      f32x4 acc0 = {0, 0, 0, 0}, acc1 = {0, 0, 0, 0};
      int rem = len - 4 * u0;
      const int a = lane >> 4;
      asm volatile("s_waitcnt lgkmcnt(0)" : "+s"(tstart));
      const unsigned t0 = ((unsigned)tstart + LEAD) << 8;
#pragma unroll 1
      for (int i = 1, jn0 = cnt; i <= nit; ++i) {
        float x[GN];
        const int cntn = i < nit ? (i == ip ? shortn : GN) : 0;
        const int jb = GN * (i < ip ? i : i == ip ? nit - 1 : i - 1);
        if (i == G0) {
          static_assert(NT == 3, "operand list below");
          int pend; unsigned v[NT], ad[NT];
#pragma unroll
          for (int k = 0; k < NT; ++k) ad[k] = (unsigned)(uintptr_t)(LAS unsigned short*)lst + 2 * min(BLK * ((lane + 64 * k) / GN) + GN * (u0 >> 2) + lane % GN, NBPS - 1);
          do asm volatile("ds_read_b32 %0, %4\n\tds_read_u16 %1, %5\n\tds_read_u16 %2, %6\n\tds_read_u16 %3, %7\n\ts_waitcnt lgkmcnt(0)"
                          : "=&v"(pend), "=&v"(v[0]), "=&v"(v[1]), "=&v"(v[2]) : "v"((unsigned)(uintptr_t)(LAS float*)(lds + NW * G * 134 + 1)), "v"(ad[0]), "v"(ad[1]), "v"(ad[2]) : "memory");
          while (__builtin_amdgcn_readfirstlane(pend) < 0);
#pragma unroll
          for (int k = 0; k < NT; ++k) {
            const int p = (u0 + UST * (lane + 64 * k)) >> 2;
            tb[k] = p >= P0 && p < pend ? v[k] * PAGE_BYTES : tb[k];
          }
          __builtin_amdgcn_sched_barrier(0);
        }
        u64 now; asm volatile("s_memrealtime %0" : "=s"(now));
        __builtin_amdgcn_sched_barrier(0);
#pragma unroll
        for (int t = 0; t < GN; ++t) {
          const int jt = jb + t;
          unsigned o = __builtin_amdgcn_readlane(tb[0], jt & 63);
#pragma unroll
          for (int k = 1; k < NT; ++k) { const unsigned ok = __builtin_amdgcn_readlane(tb[k], jt & 63); o = jt >= 64 * k ? ok : o; }
          so[t] = o; nr[t] = t < cntn ? kvbytes : 0u;
        }
        SETTLE();
        asm volatile("s_waitcnt lgkmcnt(0)" : "+s"(now));
        const unsigned due = cntn ? t0 + TAUC * jn0 + EXS * min(jn0, G0 * GMAX) : t0;
        while ((int)(((unsigned)now << 8) - due) < 0) asm volatile("s_sleep 1\n\ts_memrealtime %0\n\ts_waitcnt lgkmcnt(0)" : "=s"(now));
#pragma unroll
        for (int t = 0; t < GN; ++t) {
          WAIT(kb[t]);
          const f32x4 d = __builtin_amdgcn_mfma_f32_16x16x32_bf16(__builtin_bit_cast(bf16x8, kb[t]), qf, f32x4{0, 0, 0, 0}, 0, 0, 0);
          __builtin_amdgcn_sched_barrier(0);
          LOAD(kb[t], voffK, ka, t);
          __builtin_amdgcn_sched_barrier(0);
          float y = bank_sel<3>(bank_sel<2>(bank_sel<1>(d[0], d[1]), d[2]), d[3]);
          y += dpp<0x124>(y); y += dpp<0x128>(y);
          x[t] = a < (t < cnt ? rem - 4 * UST * t : 0) ? y * c2 : -INFINITY;
        }
        rem = len - 4 * (u0 + UST * jb); cnt = cntn; jn0 += cntn;
        float pm = x[0];
#pragma unroll
        for (int t = 1; t < GN; ++t) pm = fmaxf(pm, x[t]);
        pm = xrow_max(pm);
        if (__any(pm > m)) {
          const float mn = fmaxf(m, pm), al = __builtin_amdgcn_exp2f(m - mn);
          m = mn; l *= al; acc0 *= al; acc1 *= al;
        }
        unsigned w0[GN], w1[GN];
#pragma unroll
        for (int t = 0; t < GN; ++t) { const float p = __builtin_amdgcn_exp2f(x[t] - m); l += p; const unsigned pp = cvtpk(p, p); w0[t] = pp & mask0; w1[t] = pp & mask1; }
#pragma unroll
        for (int t = 0; t < GN; ++t) {
          WAIT(vb[t]);
          const s16x4 b = __builtin_bit_cast(s16x4, u32x2{w0[t], w1[t]});
          acc0 = __builtin_amdgcn_mfma_f32_16x16x16bf16_1k(__builtin_bit_cast(s16x4, u32x2{vb[t][0], vb[t][1]}), b, acc0, 0, 0, 0);
          acc1 = __builtin_amdgcn_mfma_f32_16x16x16bf16_1k(__builtin_bit_cast(s16x4, u32x2{vb[t][2], vb[t][3]}), b, acc1, 0, 0, 0);
          __builtin_amdgcn_sched_barrier(0);
          LOAD(vb[t], voffV, va, t);
          __builtin_amdgcn_sched_barrier(0);
        }
      }
      l = xrow_sum(l);
      float* rec = lds + (wave * G + (lane & 3)) * 132 + 32 * a + c;
#pragma unroll
      for (int r = 0; r < 4; ++r) { rec[8 * r] = acc0[r]; rec[8 * r + 4] = acc1[r]; }
      if (lane < 4) *reinterpret_cast<float2*>(lds + NW * G * 132 + (wave * G + lane) * 2) = make_float2(m, l);
      asm volatile("s_waitcnt vmcnt(0)");
#pragma unroll
      for (int t = 0; t < GN; ++t) asm volatile("" : "+v"(kb[t]), "+v"(vb[t]));
    };
    stream(std::integral_constant<int, GQ>{});
  }
  __syncthreads();
  const unsigned tag = __builtin_amdgcn_readfirstlane(__float_as_uint(lds[NW * G * 134]));
  {
    static_assert(NWT == G, "one wave per q head");
    const int g = wave;
    float mw[NW], lw[NW], M = -3.0e38f; float2 ow[NW];
#pragma unroll
    for (int w = 0; w < NW; ++w) { const float2 t = *reinterpret_cast<const float2*>(lds + NW * G * 132 + (w * G + g) * 2); mw[w] = t.x; lw[w] = t.y; ow[w] = *reinterpret_cast<const float2*>(lds + (w * G + g) * 132 + lane * 2); M = fmaxf(M, t.x); }
    float L = 0.f, o0 = 0.f, o1 = 0.f;
#pragma unroll
    for (int w = 0; w < NW; ++w) { const float f = __builtin_amdgcn_exp2f(mw[w] - M); L = fmaf(f, lw[w], L); o0 = fmaf(f, ow[w].x, o0); o1 = fmaf(f, ow[w].y, o1); }
    const float r = L > 0.f ? __builtin_amdgcn_rcpf(L) : 0.f;
    const unsigned lse = __float_as_uint(M + __builtin_amdgcn_logf(L));
    __builtin_amdgcn_raw_buffer_store_b128(u32x4{__float_as_uint(o0 * r), __builtin_amdgcn_perm(tag, lse, 0x07060302), __float_as_uint(o1 * r), __builtin_amdgcn_perm(tag, lse, 0x07060100)},
                                           wr, gv + g * 8 * NS * 128, 0, 16);
  }
  if (wave == NW) {
    combine_job(ws, h, s, tag, out, lane);
    if (s == 0 && lane == 0) __hip_atomic_store(ep, tag, __ATOMIC_RELAXED, __HIP_MEMORY_SCOPE_AGENT);
  }
  asm volatile("9:");
}

extern "C" void kernel_launch(void* const* d_in, const int* in_sizes, int n_in, void* d_out, int out_size, void* d_ws, size_t ws_size, hipStream_t stream) {
  attn_main<<<256, NWT * 64, 0, stream>>>((const char*)d_in[0], (const char*)d_in[1], (const char*)d_in[2], (const int*)d_in[3], (const int*)d_in[4],
                                         (__hip_bfloat16*)d_out, (char*)d_ws, (unsigned)in_sizes[1] * 2u);
}
```
